```python
import torch

def _softmax(x, axis=-1):
    return torch.softmax(x, dim=axis)
def _gelu(x):
    return torch.nn.functional.gelu(x, approximate="tanh")
def _silu(x):
    return torch.nn.functional.silu(x)
def _sigmoid(x):
    return torch.sigmoid(x)
def _layernorm(x, axis=-1, eps=1e-5):
    m = x.mean(dim=axis, keepdim=True)
    v = x.var(dim=axis, keepdim=True, unbiased=False)
    return (x - m) / torch.sqrt(v + eps)
def _scatter_add(tgt, idx, src, axis=0):
    return tgt.clone().index_add_(axis, idx, src)
def _segment_sum(src, seg, n):
    out = torch.zeros((n,) + src.shape[1:], dtype=src.dtype)
    return out.index_add_(0, seg, src)
def _pad(x, pw):
    flat = tuple(v for pair in reversed(pw) for v in pair)
    return torch.nn.functional.pad(x, flat)

kernel_name = 'interleave_layers__grouped_kv_mqa__moe_arch_mod__prompt'

def setup_inputs():

    _w = (lambda a: (lambda t: t.to(torch.bfloat16) if t.is_floating_point() else t)(torch.as_tensor(a)))
    def _A_setup():
        B, S, D, H = 1, 2048, 576, 32
        Dh = D // H
        return {
            "x": _w((lambda *s: torch.randn(*s, dtype=torch.bfloat16) * 0.05)(B, S, D)),
            "wq": _w((lambda *s: torch.randn(*s, dtype=torch.bfloat16) * 0.05)(D, H * Dh)),
            "wk": _w((lambda *s: torch.randn(*s, dtype=torch.bfloat16) * 0.05)(D, Dh)),
            "wv": _w((lambda *s: torch.randn(*s, dtype=torch.bfloat16) * 0.05)(D, Dh)),
            "wo": _w((lambda *s: torch.randn(*s, dtype=torch.bfloat16) * 0.05)(H * Dh, D)),
        }
    _A_in = _A_setup()

    def _B_setup():
        B, S, D, F, C = 1, 2048, 576, 4608, 1
        return {
            "x": _w((lambda *s: torch.randn(*s, dtype=torch.bfloat16) * 0.05)(B, S, D)),
            "w_r": _w((lambda *s: torch.randn(*s, dtype=torch.bfloat16) * 0.05)(D)),
            "w_gate": _w((lambda *s: torch.randn(*s, dtype=torch.bfloat16) * 0.05)(D, F)),
            "w_up": _w((lambda *s: torch.randn(*s, dtype=torch.bfloat16) * 0.05)(D, F)),
            "w_down": _w((lambda *s: torch.randn(*s, dtype=torch.bfloat16) * 0.05)(F, D)),
        }
    _B_in = _B_setup()

    return {
        "A_x": _A_in["x"],
        "A_wq": _A_in["wq"],
        "A_wk": _A_in["wk"],
        "A_wv": _A_in["wv"],
        "A_wo": _A_in["wo"],
        "B_x": _B_in["x"],
        "B_w_r": _B_in["w_r"],
        "B_w_gate": _B_in["w_gate"],
        "B_w_up": _B_in["w_up"],
        "B_w_down": _B_in["w_down"],
    }

def reference(A_x, A_wq, A_wk, A_wv, A_wo, B_x, B_w_r, B_w_gate, B_w_up, B_w_down):
    def _A_ref(x, wq, wk, wv, wo):
        B, S, D = x.shape
        H = 32
        Dh = D // H
        q = (lambda x, *d: x.permute(*d))((x @ wq).reshape(B, S, H, Dh), 0, 2, 1, 3)
        k = x @ wk
        v = x @ wv
        scores = torch.einsum("bhqd,bkd->bhqk", q, k) * (Dh ** -0.5)
        p = _softmax(scores, axis=-1)
        o = torch.einsum("bhqk,bkd->bhqd", p, v)
        return (lambda x, *d: x.permute(*d))(o, 0, 2, 1, 3).reshape(B, S, H * Dh) @ wo

    def _B_ref(x, w_r, w_gate, w_up, w_down):
        B, S, D = x.shape
        C = 1
        r = x @ w_r
        idx = torch.argsort(-r, axis=-1)[:, :C]
        idx_e = (lambda x, n, axis: torch.repeat_interleave(x, n, dim=axis))(idx.reshape(B, C, 1), D, axis=2)
        x_sel = (lambda x, i, axis: torch.take_along_dim(x, i, dim=axis))(x, idx_e, axis=1)
        g = _silu(x_sel @ w_gate)
        u = x_sel @ w_up
        y = (g * u) @ w_down
        r_sel = _sigmoid((lambda x, i, axis: torch.take_along_dim(x, i, dim=axis))(r, idx, axis=1))
        y = y * r_sel.reshape(B, C, 1)
        b_off = (torch.arange(B) * S).reshape(B, 1)
        flat_idx = (idx + b_off).reshape(B * C)
        zero = (x * 0.0).reshape(B * S, D)
        upd = _scatter_add(zero, flat_idx, y.reshape(B * C, D), axis=0)
        return x + upd.reshape(B, S, D)

    x = A_x
    x = x + _A_ref(_layernorm(x, axis=-1), A_wq, A_wk, A_wv, A_wo)
    x = x + _B_ref(_layernorm(x, axis=-1), B_w_r, B_w_gate, B_w_up, B_w_down)
    x = x + _A_ref(_layernorm(x, axis=-1), A_wq, A_wk, A_wv, A_wo)
    x = x + _B_ref(_layernorm(x, axis=-1), B_w_r, B_w_gate, B_w_up, B_w_down)
    return x
```

```cpp
#include <hip/hip_runtime.h>
#include <hip/hip_bf16.h>
#include <stdint.h>

typedef float f32x2 __attribute__((ext_vector_type(2)));
typedef float f32x4 __attribute__((ext_vector_type(4)));
typedef float f32x16 __attribute__((ext_vector_type(16)));
typedef short s16x8 __attribute__((ext_vector_type(8)));
typedef short s16x4 __attribute__((ext_vector_type(4)));
typedef unsigned u32x4 __attribute__((ext_vector_type(4)));
typedef unsigned u32x2 __attribute__((ext_vector_type(2)));
typedef __bf16 bf16x2_t __attribute__((ext_vector_type(2)));
typedef short v4i16_t __attribute__((ext_vector_type(4)));
#define LAS __attribute__((address_space(3)))
#define DI __device__ __forceinline__
#define SBAR() __builtin_amdgcn_sched_barrier(0)
#define MFMA32(a, b, c) __builtin_amdgcn_mfma_f32_32x32x16_bf16(a, b, c, 0, 0, 0)
#define MFMA32K8(a, b, c) __builtin_amdgcn_mfma_f32_32x32x8bf16_1k(a, b, c, 0, 0, 0)
#define MFMA16(a, b, c) __builtin_amdgcn_mfma_f32_16x16x32_bf16(a, b, c, 0, 0, 0)

constexpr int T = 2048, D = 576, FF = 4608;
constexpr int ROWB = D * 2;
constexpr float EPS = 1e-5f;
constexpr float QSCALE = 0.23570226039551584f * 1.4426950408889634f;
constexpr int TILE_B = 2560;
constexpr int NFF = 192, FCOL = 24;
constexpr int NRED = 36;
constexpr unsigned SENT = 0xFFFFFFFFu;
constexpr unsigned SPIN_CAP = 30000u;
constexpr int OOB = 0x7ffffff0;

constexpr size_t WS_SALT = 0;
constexpr size_t WS_FLAG = 256;
constexpr size_t WS_KV1 = WS_FLAG + 256 * 128;
constexpr size_t WS_KV2 = WS_KV1 + 64 * TILE_B;
constexpr size_t WS_O1 = WS_KV2 + 64 * TILE_B;
constexpr size_t WS_O2 = WS_O1 + (size_t)T * ROWB;
constexpr size_t WS_X1 = WS_O2 + (size_t)T * ROWB;
constexpr size_t WS_CROW = WS_X1 + (size_t)T * ROWB;
constexpr size_t WS_ST1 = WS_CROW + 32 * ROWB;
constexpr size_t WS_ST2 = WS_ST1 + 256 * 128 * 16;
constexpr size_t WS_CAND1 = WS_ST2 + 256 * 128 * 16;
constexpr size_t WS_CAND2 = WS_CAND1 + 512;
constexpr size_t WS_YP1 = WS_CAND2 + 512;
constexpr size_t WS_YP2 = WS_YP1 + NFF * D * 4;
constexpr size_t WS_X2SEL = WS_YP2 + NFF * D * 4;
constexpr size_t WS_KVSEL = WS_X2SEL + 1280;
constexpr size_t WS_QSEL = WS_KVSEL + 256;
constexpr size_t WS_FFL = WS_QSEL + 8 * 160;
constexpr size_t WS_END = WS_FFL + (size_t)NFF * 8 * 12 * 1024;
#define LAYOUT_FL 1
constexpr size_t WS_STAMP = 128u << 20;

constexpr int L_W = 0;
constexpr int L_Q = 92224;
constexpr int L_ACC = L_Q + 10240;
constexpr int L_OST = L_ACC + 24576;
constexpr int L_XT = L_OST + 9216;
constexpr int L_CNT = L_XT + 9216;
constexpr int L_QM = L_CNT + 128;
constexpr int L_STAT = L_QM + 2048;
constexpr int L_SP = L_STAT + 1024;
constexpr int L_XS = L_SP + 8192;
constexpr int L_XR = L_XS + 1152;
constexpr int L_H = L_XR + 1152;
constexpr int L_CS = L_H + 64;
constexpr int L_KS = L_CS + 2880;
constexpr int L_WR = L_KS + 128;
constexpr int L_MISC = L_WR + 144 + 16;
constexpr int LDS_BYTES = L_MISC + 64;
static_assert(LDS_BYTES <= 163840, "LDS");
enum { C_FALL = 8, C_A = 9, C_B = 10, C_R = 11, C_ONE = 12, C_QOK = 13, C_KS = 14, C_A2 = 15, C_R2 = 16, C_S5 = 17, C_XS = 18, C_H = 19, C_H2 = 20 };

struct Params {
    const char* x; const char* wq; const char* wk; const char* wv; const char* wo;
    const char* wr; const char* wg; const char* wu; const char* wd;
    char* out; char* ws;
};

#define STAMP(i) asm volatile("; MARK " #i)
#define WSTAMP(slot, val) do { } while (0)
DI unsigned cvtpk(float lo, float hi) { f32x2 v = {lo, hi}; bf16x2_t b = __builtin_convertvector(v, bf16x2_t); return __builtin_bit_cast(unsigned, b); }
DI float bflo(unsigned w) { return __uint_as_float(w << 16); }
DI float bfhi(unsigned w) { return __uint_as_float(w & 0xffff0000u); }
DI float rbf(float x) { return bflo(cvtpk(x, x)); }
DI unsigned nosent(unsigned w) { return w < SENT - 1 ? w : SENT - 1; }
DI u32x4 nosent4(u32x4 v) { return u32x4{nosent(v[0]), nosent(v[1]), nosent(v[2]), nosent(v[3])}; }
DI float dot2(unsigned a, unsigned b, float c) { return __builtin_amdgcn_fdot2_f32_bf16(__builtin_bit_cast(bf16x2_t, a), __builtin_bit_cast(bf16x2_t, b), c, false); }
DI void glds16(const void* g, unsigned lds_base) {
    unsigned sv; asm volatile("s_mov_b32 %0, m0\n\ts_mov_b32 m0, %2\n\ts_nop 0\n\tglobal_load_lds_dwordx4 %1, off\n\ts_mov_b32 m0, %0" : "=&s"(sv) : "v"(g), "s"(lds_base) : "memory"); }
DI void wait_vm0() { __builtin_amdgcn_s_waitcnt(0x0F70); asm volatile("" ::: "memory"); }
DI void wait_lg0() { asm volatile("s_waitcnt lgkmcnt(0)" ::: "memory"); }
DI void bar() { asm volatile("s_waitcnt lgkmcnt(0)\n\ts_barrier" ::: "memory"); }
typedef LAS char* lptr;
DI s16x4 trd(lptr p) { return __builtin_bit_cast(s16x4, __builtin_amdgcn_ds_read_tr16_b64_v4i16((LAS v4i16_t*)p)); }
DI s16x8 tr2(lptr p0, lptr p1) { s16x4 a = trd(p0), b = trd(p1); return __builtin_shufflevector(a, b, 0, 1, 2, 3, 4, 5, 6, 7); }
constexpr int SC1 = 16;
DI __amdgpu_buffer_rsrc_t mkrs(const void* p, unsigned bytes) { return __builtin_amdgcn_make_buffer_rsrc((void*)p, 0, bytes, 0x00020000); }
DI u32x4 ld128c(__amdgpu_buffer_rsrc_t rs, int off) { return __builtin_amdgcn_raw_buffer_load_b128(rs, off, 0, SC1); }
DI u32x2 ld64c(__amdgpu_buffer_rsrc_t rs, int off) { return __builtin_amdgcn_raw_buffer_load_b64(rs, off, 0, SC1); }
DI unsigned ld32c(__amdgpu_buffer_rsrc_t rs, int off) { return __builtin_amdgcn_raw_buffer_load_b32(rs, off, 0, SC1); }
DI void st128c(__amdgpu_buffer_rsrc_t rs, int off, u32x4 v) { __builtin_amdgcn_raw_buffer_store_b128(v, rs, off, 0, SC1); }
DI void st64c(__amdgpu_buffer_rsrc_t rs, int off, u32x2 v) { __builtin_amdgcn_raw_buffer_store_b64(v, rs, off, 0, SC1); }
DI void st32c(__amdgpu_buffer_rsrc_t rs, int off, unsigned v) { __builtin_amdgcn_raw_buffer_store_b32(v, rs, off, 0, SC1); }
DI unsigned umax(unsigned a, unsigned b) { return a > b ? a : b; }
DI unsigned umax4(u32x4 v) { return umax(umax(v[0], v[1]), umax(v[2], v[3])); }
DI bool spin(unsigned& n) { __builtin_amdgcn_s_sleep(1); return ++n > SPIN_CAP; }
DI bool spin_long(unsigned& n) { __builtin_amdgcn_s_sleep(12); return ++n > SPIN_CAP; }
DI u32x4 poll128(__amdgpu_buffer_rsrc_t rs, int off) {
    u32x4 v;
    for (unsigned n = 0;;) { v = ld128c(rs, off); if (!__any(umax4(v) == SENT) || spin(n)) break; }
    return v;
}
DI void hint_wait(__amdgpu_buffer_rsrc_t rs, int off) { for (unsigned n = 0; __any(ld32c(rs, off) == SENT) && n < SPIN_CAP; ++n) __builtin_amdgcn_s_sleep(4); }
DI int wrow(int k) { return k ^ (((k >> 3) & 1) << 2); }
DI float sum4g(float x) {
    const auto a = __builtin_amdgcn_permlane16_swap(__float_as_uint(x), __float_as_uint(x), false, false); x = __uint_as_float(a[0]) + __uint_as_float(a[1]);
    const auto b = __builtin_amdgcn_permlane32_swap(__float_as_uint(x), __float_as_uint(x), false, false); return __uint_as_float(b[0]) + __uint_as_float(b[1]); }
#define ROR(x, n) __uint_as_float(__builtin_amdgcn_update_dpp(0, __float_as_uint(x), 0x120 + (n), 0xf, 0xf, false))
#define QUAD(x, c) __uint_as_float(__builtin_amdgcn_update_dpp(0, __float_as_uint(x), c, 0xf, 0xf, false))
DI float wmax(float x) {
    x = fmaxf(x, ROR(x, 8)); x = fmaxf(x, ROR(x, 4)); x = fmaxf(x, ROR(x, 2)); x = fmaxf(x, ROR(x, 1));
    const auto a = __builtin_amdgcn_permlane16_swap(__float_as_uint(x), __float_as_uint(x), false, false); x = fmaxf(__uint_as_float(a[0]), __uint_as_float(a[1]));
    const auto b = __builtin_amdgcn_permlane32_swap(__float_as_uint(x), __float_as_uint(x), false, false); return fmaxf(__uint_as_float(b[0]), __uint_as_float(b[1])); }
DI float wsum(float x) { x += ROR(x, 8); x += ROR(x, 4); x += ROR(x, 2); x += ROR(x, 1); return sum4g(x); }
DI float rdl(float x, int lane) { return __uint_as_float(__builtin_amdgcn_readlane(__float_as_uint(x), lane)); }
DI void arrive(lptr lb, int c, int l) { wait_lg0(); if (l == 0) __hip_atomic_fetch_add((LAS int*)(lb + L_CNT) + c, 1, __ATOMIC_RELAXED, __HIP_MEMORY_SCOPE_WORKGROUP); }
DI void lds_wait_ge(volatile LAS int* p, int v) { for (unsigned n = 0; *p < v && n < (1u << 20); ++n) __builtin_amdgcn_s_sleep(1); asm volatile("" ::: "memory"); }
DI void await8(lptr lb, int c) { lds_wait_ge((volatile LAS int*)(lb + L_CNT) + c, 8); }

DI const char* uptr(const char* p) {
    const unsigned long long v = (unsigned long long)p;
    return (const char*)(((unsigned long long)(unsigned)__builtin_amdgcn_readfirstlane((unsigned)(v >> 32)) << 32) | (unsigned)__builtin_amdgcn_readfirstlane((unsigned)v)); }
DI void glds16s(const char* sbase, unsigned voff, unsigned lds_base, unsigned long long mask) {
    unsigned sv;
    asm volatile("s_mov_b32 %0, m0\n\ts_mov_b32 m0, %3\n\ts_mov_b64 exec, %4\n\tglobal_load_lds_dwordx4 %1, %2\n\ts_mov_b64 exec, -1\n\ts_mov_b32 m0, %0"
                 : "=&s"(sv) : "v"(voff), "s"(uptr(sbase)), "s"(lds_base), "s"(mask) : "memory"); }
constexpr unsigned long long lanes10(bool nine) { unsigned long long m = 0; for (int l = 0; l < 60; ++l) if ((l % 10 == 9) == nine) m |= 1ull << l; return m; }
DI unsigned w_voff(int w, int l) { const int r = (l * 26) >> 8; return wrow(6 * w + r) * ROWB + (l - r * 10) * 16; }
DI void dma_w_piece(unsigned ldsw, const char* wsl, unsigned voff, int w, int i) {
    glds16s(wsl + i * (48 * ROWB), voff, (unsigned)__builtin_amdgcn_readfirstlane(ldsw + (w + 8 * i) * 960), lanes10(false)); }
DI void dma_w(unsigned ldsw, const char* wsl, int w, int l) {
    const unsigned voff = w_voff(w, l);
#pragma unroll
    for (int i = 0; i < 12; ++i) dma_w_piece(ldsw, wsl, voff, w, i);
}
DI void dma_ffn_frag(unsigned slot, const char* wg, const char* wu, const char* wd, int w, int l, int j) {
    const int row = l >> 1, half = l & 1;
    if (j < 7) { const int s = j < 6 ? 2 * w + j / 3 : 16 + (w & 1), nt = j < 6 ? j % 3 : w >> 1; const unsigned voff = row * (FF * 2) + half * 16; const size_t so = (size_t)s * 32 * (FF * 2);
        if (j < 6 || w < 6) {
            if (nt == 1) { glds16s(wg + so + 32, voff, slot, 0x5555555555555555ull); glds16s(wu + so - 16, voff, slot, 0xaaaaaaaaaaaaaaaaull); }
            else glds16s((nt == 0 ? wg : wu + 16) + so, voff, slot, ~0ull); } }
    else { const int nt = w + 8 * (j - 7);
        if (nt < 36) glds16s(wd + nt * 32, (row < FCOL ? row : row - 8) * ROWB + half * 16, slot, ~0ull); }
}
DI void wimg_ptrs(char* lds, int kh, int l, lptr& wa, lptr& wb) {
    const int g = l >> 4, q = (l & 15) >> 2, o = g & 1;
    lptr base = (lptr)lds + L_W + kh * 9 * 5120 + (l & 3) * 8;
    wa = base + (8 * g + 4 * o + q) * 160; wb = base + (8 * g + 4 * (1 - o) + q) * 160;
}
#define WFRAG(s, nt) tr2(wa + (s) * 5120 + (nt) * 32, wb + (s) * 5120 + (nt) * 32)
DI void gemm_half(f32x4 (&acc)[5], const u32x4 (&xf)[9], lptr wa, lptr wb) {
    s16x8 a0 = WFRAG(0, 0), a1 = WFRAG(0, 1), a2 = WFRAG(0, 2), a3 = WFRAG(0, 3), a4 = WFRAG(0, 4);
#pragma unroll
    for (int s = 0; s < 9; ++s) {
        s16x8 n0 = a0, n1 = a1, n2 = a2, n3 = a3, n4 = a4;
        const s16x8 xb = __builtin_bit_cast(s16x8, xf[s]);
        if (s < 8) { n0 = WFRAG(s + 1, 0); n1 = WFRAG(s + 1, 1); }
        acc[0] = MFMA16(a0, xb, acc[0]); SBAR();
        if (s < 8) { n2 = WFRAG(s + 1, 2); }
        acc[1] = MFMA16(a1, xb, acc[1]); SBAR();
        if (s < 8) { n3 = WFRAG(s + 1, 3); }
        acc[2] = MFMA16(a2, xb, acc[2]); SBAR();
        if (s < 8) { n4 = WFRAG(s + 1, 4); }
        acc[3] = MFMA16(a3, xb, acc[3]); SBAR();
        acc[4] = MFMA16(a4, xb, acc[4]); SBAR();
        a0 = n0; a1 = n1; a2 = n2; a3 = n3; a4 = n4;
    }
}
DI void khalf_send(char* lds, f32x4 a0, f32x4 a1, f32x4 a2, f32x4 a3, f32x4 a4, int mt, int kh, int l) {
    LAS f32x4* red = (LAS f32x4*)((lptr)lds + L_ACC) + mt * 5 * 64 + l;
    if (kh == 0) { red[3 * 64] = a3; red[4 * 64] = a4; } else { red[0] = a0; red[64] = a1; red[128] = a2; }
}
DI f32x4 sel4(bool c, f32x4 a, f32x4 b) { return f32x4{c ? a[0] : b[0], c ? a[1] : b[1], c ? a[2] : b[2], c ? a[3] : b[3]}; }
DI void khalf_recv(char* lds, f32x4 a0, f32x4 a1, f32x4 a2, f32x4 a3, f32x4 a4, f32x4& o0, f32x4& o1, f32x4& o2, int mt, int kh, int l) {
    LAS f32x4* red = (LAS f32x4*)((lptr)lds + L_ACC) + mt * 5 * 64 + l + kh * 3 * 64;
    const f32x4 r0 = red[0], r1 = red[64], r2 = red[kh ? 0 : 128];
    o0 = sel4(kh != 0, a3, a0) + r0; o1 = sel4(kh != 0, a4, a1) + r1; o2 = a2 + r2;
}
#define KSEND(acc) khalf_send(lds, acc[0], acc[1], acc[2], acc[3], acc[4], mt, kh, l)
#define KRECV(acc, own) khalf_recv(lds, acc[0], acc[1], acc[2], acc[3], acc[4], own[0], own[1], own[2], mt, kh, l)
DI void publish_kv(char* lds, char* kvbuf, int tb, int hg, int tid) {
    const __amdgpu_buffer_rsrc_t rs = mkrs(kvbuf, 64 * TILE_B);
    lptr q = (lptr)lds + L_Q;
    if (tid < 64) {
        const int tile = 2 * tb + (tid >> 5), key = tid & 31;
        if (hg < 2) st128c(rs, tile * TILE_B + hg * 512 + key * 16, nosent4(*(LAS u32x4*)(q + tid * 160 + 144)));
        else if (hg == 2) st64c(rs, tile * TILE_B + 1024 + key * 8, u32x2{nosent(*(LAS unsigned*)(q + tid * 160 + 144)), 0u});
    } else if (tid < 128 && hg >= 2 && hg <= 4) {
        const int idx = tid - 64, c8 = idx & 7, dl = idx >> 3, tt = c8 >> 2, s = (c8 >> 1) & 1, h = c8 & 1;
        const int d = hg == 2 ? dl - 2 : (hg == 3 ? 6 + dl : 14 + dl);
        if (d >= 0 && d < 20) {
            u32x4 v;
            if (d < 18) {
#pragma unroll
                for (int j = 0; j < 4; ++j) {
                    const int k0 = 32 * tt + 16 * s + 8 * (j >> 1) + 4 * h + 2 * (j & 1);
                    const unsigned lo = *(LAS unsigned short*)(q + k0 * 160 + 144 + dl * 2), hi = *(LAS unsigned short*)(q + (k0 + 1) * 160 + 144 + dl * 2);
                    v[j] = nosent(lo | (hi << 16));
                }
            } else { const unsigned c = d == 18 ? 0x3f803f80u : 0u; v = u32x4{c, c, c, c}; }
            st128c(rs, (2 * tb + tt) * TILE_B + 1280 + s * 640 + h * 320 + d * 16, v);
        }
    }
}
DI void proj_epilogue(char* lds, const f32x4 (&own)[3], lptr csum, float mean, float c, int mt, int kh, int l) {
    const int g = l >> 4, tl = l & 15;
    lptr row = (lptr)lds + L_Q + (mt * 16 + tl) * 160 + g * 8 + kh * 96;
    lptr cp = csum + (kh * 48 + g * 4) * 4;
#pragma unroll
    for (int i = 0; i < 3; ++i) {
        if (i < 2 || kh == 0) {
            const f32x4 cs = *(LAS f32x4*)(cp + i * 64);
            const float sc = (kh == 1 && i == 1 && g >= 2) ? c : c * QSCALE;
            f32x4 v;
#pragma unroll
            for (int r = 0; r < 4; ++r) v[r] = (own[i][r] - mean * cs[r]) * sc;
            *(LAS u32x2*)(row + i * 32) = u32x2{cvtpk(v[0], v[1]), cvtpk(v[2], v[3])};
        }
    }
}

typedef __attribute__((address_space(4))) const Params* KP;
DI KP kargs() { KP k = (KP)__builtin_amdgcn_kernarg_segment_ptr(); asm volatile("" : "+s"(k)); return k; }
DI int fresh_tid() { int t = threadIdx.x; asm volatile("" : "+v"(t)); return t; }
#define IDS() const int tid = fresh_tid(), l = tid & 63; const int w = __builtin_amdgcn_readfirstlane(tid >> 6); const int b = blockIdx.x; \
    const int tb = (b & 7) + 8 * (b >> 6), hg = (b >> 3) & 7, mt = w & 3, kh = w >> 2, g = l >> 4, tl = l & 15; lptr lb = (lptr)lds; \
    const unsigned lds0 = (unsigned)(uintptr_t)lds; const KP p = kargs(); char* const ws = p->ws; const __amdgpu_buffer_rsrc_t rs = mkrs(ws, (unsigned)WS_END); \
    const int tok = tb * 64 + mt * 16 + tl; const bool ffn = (b >> 3) < 24; const int fsl = (b & 7) * 24 + (b >> 3); const int tile0 = tb * 64 * ROWB + hg * 144; const int frag0 = ((tb * 4 + mt) * 18 + kh * 9) * 1024 + l * 16; \
    (void)hg; (void)g; (void)tok; (void)lds0; (void)lb; (void)kh; (void)rs; (void)ffn; (void)fsl; (void)tl; (void)mt; (void)tile0; (void)frag0
struct Park { u32x4 v[12]; };
DI s16x8 pfrag(const Park& Z, int j) { return __builtin_bit_cast(s16x8, Z.v[j]); }
DI void img_get(Park& Z, lptr lb, int w, int l) {
#pragma unroll
    for (int i = 0; i < 12; ++i) Z.v[i] = *(LAS u32x4*)(lb + L_W + (w + 8 * i) * 960 + l * 16); }
DI void img_put(const Park& Z, lptr lb, int w, int l) {
    if (l < 60) {
#pragma unroll
        for (int i = 0; i < 12; ++i) *(LAS u32x4*)(lb + L_W + (w + 8 * i) * 960 + l * 16) = Z.v[i]; } }
DI int chunk_off(int c) { return c * 16 + ((c * 7282) >> 16) * (ROWB - 144); }
DI int fl_off(int t, int cc) { return ((t >> 4) * 18 + (cc >> 2)) * 1024 + ((cc & 3) * 16 + (t & 15)) * 16; }
DI int fl_tile(int tb, int hg, int ch, int l) { const int cc = hg * 9 + ch; return (tb * 4 * 18 + (cc >> 2)) * 1024 + (cc & 3) * 256 + (l >> 4) * 18432 + (l & 15) * 16; }
DI unsigned long long ld_salt(__amdgpu_buffer_rsrc_t rs) { const u32x2 s = ld64c(rs, (int)WS_SALT); return ((unsigned long long)s[1] << 32) | s[0]; }

DI void attn_init(char* lds, int tid) {
    lptr lb = (lptr)lds;
    if (tid < 32) *(LAS int*)(lb + L_CNT + tid * 4) = tid == C_ONE ? 0x3f80 : 0;
    *(LAS unsigned*)(lb + L_QM + tid * 4) = 0u;
}

DI void proj1(char* lds, Park& PK) {
    IDS();
    STAMP(0);
    u32x4 xf[9];
    { const char* xr = p->x + (size_t)(tb * 64 + mt * 16 + (l >> 2)) * ROWB + kh * 576 + (l & 3) * 16;
#pragma unroll
      for (int s = 0; s < 9; ++s) xf[s] = *(const u32x4*)(xr + s * 64); }
    dma_w(lds0 + L_W, p->wq + hg * 144, w, l);
    const unsigned long long tag = ld_salt(rs) + 1;
    { typedef unsigned u32x4u __attribute__((ext_vector_type(4), aligned(4))); typedef unsigned u32x3u __attribute__((ext_vector_type(3), aligned(4)));
      const int k1 = 512 + l;
      u32x4 v0 = {0u, 0u, 0u, 0u}, v1 = v0;
      if (hg != 2 && hg < 5) { const char* a = hg < 2 ? p->wk + hg * 16 : p->wv + (hg == 3 ? 12 : 20);
          v0 = *(const u32x4u*)(a + tid * 36); if (w == 0) v1 = *(const u32x4u*)(a + k1 * 36);
          if (hg == 4) { v0 = u32x4{v0[2], v0[3], 0u, 0u}; v1 = u32x4{v1[2], v1[3], 0u, 0u}; } }
      else if (hg == 2) { const u32x3u a = *(const u32x3u*)(p->wv + tid * 36); v0 = u32x4{*(const unsigned*)(p->wk + tid * 36 + 32), a[0], a[1], a[2]};
          if (w == 0) { const u32x3u c = *(const u32x3u*)(p->wv + k1 * 36); v1 = u32x4{*(const unsigned*)(p->wk + k1 * 36 + 32), c[0], c[1], c[2]}; } }
      unsigned wrv = 0; if (w == 6 && l < 36) wrv = *(const unsigned*)(p->wr + hg * 144 + l * 4);
      float wrs = 0.f;
      if (w == 7) {
#pragma unroll
          for (int i = 0; i < 9; ++i) wrs += bflo(((const unsigned short*)p->wr)[l + 64 * i]); }
      *(LAS u32x4*)(lb + L_W + wrow(tid) * 160 + 144) = v0;
      if (w == 0) *(LAS u32x4*)(lb + L_W + wrow(k1) * 160 + 144) = v1;
      if (w == 6 && l < 36) *(LAS unsigned*)(lb + L_WR + l * 4) = wrv;
      if (w == 7) { wrs = wsum(wrs); if (l == 0) *(LAS float*)(lb + L_MISC + 16) = wrs; } }
    float sx = 0.f, sxx = 0.f;
#pragma unroll
    for (int s = 0; s < 9; ++s) {
#pragma unroll
        for (int j = 0; j < 4; ++j) { sx = dot2(xf[s][j], 0x3f803f80u, sx); sxx = dot2(xf[s][j], xf[s][j], sxx); }
    }
    sx += QUAD(sx, 0xb1); sx += QUAD(sx, 0x4e); sxx += QUAD(sxx, 0xb1); sxx += QUAD(sxx, 0x4e);
    if ((l & 3) == 0) *(LAS f32x2*)(lb + L_SP + (kh * 64 + mt * 16 + (l >> 2)) * 8) = f32x2{sx, sxx};
    { const int src = (tl * 4 + g) * 4;
#pragma unroll
      for (int s = 0; s < 9; ++s) {
#pragma unroll
          for (int j = 0; j < 4; ++j) xf[s][j] = (unsigned)__builtin_amdgcn_ds_bpermute(src, (int)xf[s][j]); } }
    wait_vm0(); bar();
    STAMP(1);
    { const u32x4 s4 = {SENT, SENT, SENT, SENT};
      st128c(rs, (int)WS_O1 + fl_tile(tb, hg, w, l), s4);
      if (w == 0) st128c(rs, (int)WS_O1 + fl_tile(tb, hg, 8, l), s4);
      if (w == 1 && l < 40) st128c(rs, (int)WS_KV2 + b * 640 + l * 16, s4);
      if (b == 0 && w == 2 && l < 32) st128c(rs, (int)WS_CAND1 + l * 16, s4); }
    lptr wa, wb; wimg_ptrs(lds, kh, l, wa, wb);
    f32x4 acc[5] = {}, cs[5] = {};
    gemm_half(acc, xf, wa, wb);
    { const s16x8 ones = {0x3f80, 0x3f80, 0x3f80, 0x3f80, 0x3f80, 0x3f80, 0x3f80, 0x3f80};
      lptr ca = wa + mt * 5120, cb = wb + mt * 5120;
#pragma unroll
      for (int i = 0; i < 3; ++i) {
          if (i < 2 || mt == 0) {
#pragma unroll
              for (int nt = 0; nt < 5; ++nt) cs[nt] = MFMA16(tr2(ca + i * 4 * 5120 + nt * 32, cb + i * 4 * 5120 + nt * 32), ones, cs[nt]);
          }
      } }
    KSEND(acc);
    if (tl == 0) {
#pragma unroll
        for (int nt = 0; nt < 5; ++nt) *(LAS f32x4*)(lb + L_CS + 320 + (w * 80 + nt * 16 + g * 4) * 4) = cs[nt];
    }
    bar();
    f32x4 own[3]; KRECV(acc, own);
    if (tid < 80) { float s = 0.f;
#pragma unroll
        for (int i = 0; i < 8; ++i) s += *(LAS float*)(lb + L_CS + 320 + (i * 80 + tid) * 4);
        *(LAS float*)(lb + L_CS + tid * 4) = s; }
    const f32x2 s0 = *(LAS f32x2*)(lb + L_SP + (mt * 16 + tl) * 8), s1 = *(LAS f32x2*)(lb + L_SP + (64 + mt * 16 + tl) * 8);
    const float mean = (s0[0] + s1[0]) * (1.f / D), var = fmaxf((s0[1] + s1[1]) * (1.f / D) - mean * mean, 0.f);
    bar();
    proj_epilogue(lds, own, lb + L_CS, mean, rsqrtf(var + EPS), mt, kh, l);
    attn_init(lds, tid);
    bar();
    publish_kv(lds, ws + WS_KV1, tb, hg, tid);
    wait_vm0(); bar();
    if (tid == 0) st64c(rs, (int)WS_FLAG + b * 128, u32x2{(unsigned)tag, (unsigned)(tag >> 32)});
    STAMP(2);
    img_get(PK, lb, w, l);
    { const u32x4 s4 = {SENT, SENT, SENT, SENT}; const int o = fl_tile(tb, hg, w, l);
      st128c(rs, (int)WS_X1 + o, s4); st128c(rs, (int)WS_O2 + o, s4);
      if (w == 0) { const int o2 = fl_tile(tb, hg, 8, l); st128c(rs, (int)WS_X1 + o2, s4); st128c(rs, (int)WS_O2 + o2, s4); }
      if (w == 1 || w == 2) st128c(rs, (int)WS_ST1 + (tb * 8 + hg) * 2048 + (tid - 64) * 16, s4);
      if (w == 3 || w == 4) st128c(rs, (int)WS_ST2 + (tb * 8 + hg) * 2048 + (tid - 192) * 16, s4);
      if (ffn && tid >= 320 && tid < 464) { st128c(rs, (int)WS_YP1 + fsl * 2304 + (tid - 320) * 16, s4); st128c(rs, (int)WS_YP2 + fsl * 2304 + (tid - 320) * 16, s4); }
      if (w == 7 && l >= 32 && l < 41) st128c(rs, (int)WS_CROW + tb * ROWB + hg * 144 + (l - 32) * 16, s4);
      if (b == 0 && w == 5 && l < 32) st128c(rs, (int)WS_CAND2 + l * 16, s4);
      if (b == 1 && tid >= 320 && tid < 400) st128c(rs, (int)WS_X2SEL + (tid - 320) * 16, s4);
      if (b == 2 && w == 5 && l < 8) st128c(rs, (int)WS_KVSEL + l * 16, s4);
      if (b == 3 && tid >= 320 && tid < 400) st128c(rs, (int)WS_QSEL + (tid - 320) * 16, s4); }
    if (tid == 0) *(LAS u32x2*)(lb + L_MISC + 32) = u32x2{(unsigned)tag, (unsigned)(tag >> 32)};
    bar();
}

struct SidePtrs { const char* wg; const char* wu; const char* wd; char* ws; };
template <int I>
DI void side1_step(char* lds, const SidePtrs& P, int w, int l_) {
    const int b = blockIdx.x; int l = l_; asm volatile("" : "+v"(l));
    lptr lb = (lptr)lds; const unsigned lds0 = (unsigned)(uintptr_t)lds;
    if ((b >> 3) < 24) {
        wait_vm0();
        if constexpr (I >= 1) {
            lptr a = lb + L_OST + w * 2048 + (l >> 4) * 256 + (l & 15) * 8;
            const s16x8 f0 = tr2(a, a + 128), f1 = tr2(a + 1024, a + 1024 + 128);
            const __amdgpu_buffer_rsrc_t rs = mkrs(P.ws + WS_FFL, (unsigned)(WS_END - WS_FFL));
            const int o = ((((b & 7) * 24 + (b >> 3)) * 8 + w) * 12 + 2 * (I - 1)) * 1024;
            __builtin_amdgcn_raw_buffer_store_b128(__builtin_bit_cast(u32x4, f0), rs, l * 16, o, 0);
            __builtin_amdgcn_raw_buffer_store_b128(__builtin_bit_cast(u32x4, f1), rs, l * 16, o + 1024, 0);
        }
        if constexpr (I < 6) { const unsigned slot = (unsigned)__builtin_amdgcn_readfirstlane(lds0 + L_OST + w * 2048);
            wait_lg0(); dma_ffn_frag(slot, P.wg, P.wu, P.wd, w, l, 2 * I); dma_ffn_frag(slot + 1024, P.wg, P.wu, P.wd, w, l, 2 * I + 1); }
    }
    if constexpr (I == 6) arrive(lb, C_S5, l);
    if constexpr (I == 4) {
        if (w == 0) {
            const __amdgpu_buffer_rsrc_t rs = mkrs(P.ws, (unsigned)WS_END);
            const u32x2 tg = *(LAS u32x2*)(lb + L_MISC + 32);
            for (unsigned n = 0;;) { bool bad = false;
#pragma unroll
                for (int k = 0; k < 4; ++k) { const u32x2 f = ld64c(rs, (int)WS_FLAG + (k * 64 + l) * 128); bad = bad | (f[0] != tg[0]) | (f[1] != tg[1]); }
                if (!__any(bad) || spin(n)) break; }
            if (b == 0 && l == 0) st64c(rs, (int)WS_SALT, tg);
        }
    }
}
DI void side1(char* lds, int i, const SidePtrs& P, int w, int l) {
    if (i == 1) side1_step<1>(lds, P, w, l); else if (i == 2) side1_step<2>(lds, P, w, l); else if (i == 3) side1_step<3>(lds, P, w, l);
    else if (i == 4) side1_step<4>(lds, P, w, l); else if (i == 5) side1_step<5>(lds, P, w, l); else side1_step<6>(lds, P, w, l);
}
DI void ffn_gu(lptr lb, const Park& F, int w, int l, int cnt, int done) {
    const int g = l >> 4;
    lptr xs = lb + L_XS + g * 16;
    const s16x8 x0 = *(LAS s16x8*)(xs + w * 128), x1 = *(LAS s16x8*)(xs + w * 128 + 64), x2 = *(LAS s16x8*)(xs + (16 + (w & 1)) * 64);
    const f32x4 z = {};
    f32x4 a0 = MFMA16(pfrag(F, 0), x0, z), a1 = MFMA16(pfrag(F, 1), x0, z), a2 = MFMA16(pfrag(F, 2), x0, z);
    a0 = MFMA16(pfrag(F, 3), x1, a0); a1 = MFMA16(pfrag(F, 4), x1, a1); a2 = MFMA16(pfrag(F, 5), x1, a2);
    if (w < 2) a0 = MFMA16(pfrag(F, 6), x2, a0); else if (w < 4) a1 = MFMA16(pfrag(F, 6), x2, a1); else if (w < 6) a2 = MFMA16(pfrag(F, 6), x2, a2);
    if ((l & 15) == 0) { LAS float* pp = (LAS float*)(lb + L_SP) + (g * 4) * 8 + w;
#pragma unroll
        for (int r = 0; r < 4; ++r) { pp[r * 8] = a0[r]; pp[(16 + r) * 8] = a1[r]; pp[(32 + r) * 8] = a2[r]; } }
    wait_lg0();
    int old = 0; if (l == 0) old = __hip_atomic_fetch_add((LAS int*)(lb + L_CNT) + cnt, 1, __ATOMIC_RELAXED, __HIP_MEMORY_SCOPE_WORKGROUP);
    if (__builtin_amdgcn_readfirstlane(old) == 7) {
        const int c = l < 48 ? l : 0;
        const f32x4 p0 = *(LAS f32x4*)(lb + L_SP + c * 32), p1 = *(LAS f32x4*)(lb + L_SP + c * 32 + 16);
        const float v = rbf(((p0[0] + p0[1]) + (p0[2] + p0[3])) + ((p1[0] + p1[1]) + (p1[2] + p1[3])));
        const float u = __shfl(v, l + 24);
        const float si = rbf(v * __builtin_amdgcn_rcpf(1.f + __expf(-v)));
        if (l < 32) *(LAS unsigned short*)(lb + L_H + l * 2) = l < FCOL ? (unsigned short)(cvtpk(si * u, 0.f) & 0xffffu) : (unsigned short)0;
        wait_lg0(); if (l == 0) *((volatile LAS int*)(lb + L_CNT) + done) = 1;
    }
}
DI void ffn_dn(lptr lb, const Park& F, __amdgpu_buffer_rsrc_t rs, int oY, int w, int l, int done) {
    lds_wait_ge((volatile LAS int*)(lb + L_CNT) + done, 1);
    const s16x8 hb = *(LAS s16x8*)(lb + L_H + (l >> 4) * 16);
    const f32x4 z = {};
    f32x4 y[5];
#pragma unroll
    for (int j = 0; j < 5; ++j) y[j] = MFMA16(pfrag(F, 7 + j), hb, z);
    if ((l & 15) == 0) { const int o = oY + (w * 16 + (l >> 4) * 4) * 4;
#pragma unroll
        for (int j = 0; j < 5; ++j) if (j < 4 || w < 4) st128c(rs, o + j * 512, nosent4(__builtin_bit_cast(u32x4, y[j]))); }
}
DI bool ffn_reduce(lptr lb, __amdgpu_buffer_rsrc_t rs, int oY, int rid, int tid, int w, int cnt, unsigned& ow) {
    const int l = tid & 63, q4 = tid & 3, pr = tid >> 2;
    f32x4 s = __builtin_bit_cast(f32x4, poll128(rs, oY + pr * 2304 + rid * 64 + q4 * 16));
    if (w < 4) s += __builtin_bit_cast(f32x4, poll128(rs, oY + (pr + 128) * 2304 + rid * 64 + q4 * 16));
#pragma unroll
    for (int r = 0; r < 4; ++r) { float x = s[r]; x += ROR(x, 8); x += ROR(x, 4); s[r] = sum4g(x); }
    if (l < 4) *(LAS f32x4*)(lb + L_SP + 4096 + (w * 4 + l) * 16) = s;
    wait_lg0();
    int old = 0; if (l == 0) old = __hip_atomic_fetch_add((LAS int*)(lb + L_CNT) + cnt, 1, __ATOMIC_RELAXED, __HIP_MEMORY_SCOPE_WORKGROUP);
    if (__builtin_amdgcn_readfirstlane(old) != 7) return false;
    const f32x4 sv = *(LAS f32x4*)(lb + L_MISC);
    const float sg = rbf(__builtin_amdgcn_rcpf(1.f + __expf(-rbf(sv[0]))));
    const int c = (l & 7) * 2; f32x2 y = {0.f, 0.f};
#pragma unroll
    for (int i = 0; i < 8; ++i) y += *(LAS f32x2*)(lb + L_SP + 4096 + (i * 16 + c) * 4);
    const unsigned xr = *(LAS unsigned*)(lb + L_XR + (rid * 16 + c) * 2), xn = *(LAS unsigned*)(lb + L_XS + (rid * 16 + c) * 2);
    ow = cvtpk(bflo(xr) + rbf(bflo(xn) + rbf(rbf(y[0]) * sg)), bfhi(xr) + rbf(bfhi(xn) + rbf(rbf(y[1]) * sg)));
    return true;
}
DI int routed_off(lptr lb, int base, bool frag, int c) { const int sel = __float_as_int(*(LAS float*)(lb + L_MISC + 4)); return c < 72 ? base + (frag ? fl_off(sel, c) : (sel >> 6) * ROWB + c * 16) : OOB; }
DI void routed_row(lptr lb, __amdgpu_buffer_rsrc_t rs, int base, bool frag, int l, u32x4 x0, u32x4 x1) {
    for (unsigned n = 0; __any(umax(umax4(x0), umax4(x1)) == SENT) && !spin(n);) { x0 = ld128c(rs, routed_off(lb, base, frag, l)); x1 = ld128c(rs, routed_off(lb, base, frag, l + 64)); }
    const f32x4 sv = *(LAS f32x4*)(lb + L_MISC);
    u32x4 n0, n1;
#pragma unroll
    for (int j = 0; j < 4; ++j) { n0[j] = cvtpk((bflo(x0[j]) - sv[2]) * sv[3], (bfhi(x0[j]) - sv[2]) * sv[3]); n1[j] = cvtpk((bflo(x1[j]) - sv[2]) * sv[3], (bfhi(x1[j]) - sv[2]) * sv[3]); }
    *(LAS u32x4*)(lb + L_XR + l * 16) = x0; *(LAS u32x4*)(lb + L_XS + l * 16) = n0;
    if (l < 8) { *(LAS u32x4*)(lb + L_XR + (64 + l) * 16) = x1; *(LAS u32x4*)(lb + L_XS + (64 + l) * 16) = n1; }
}
DI bool routed_kv(lptr lb, __amdgpu_buffer_rsrc_t rs, int l, bool block) {
    unsigned v;
    for (unsigned n = 0;;) { v = ld32c(rs, l < 18 ? (int)WS_KVSEL + l * 4 : OOB); if (!__any(v == SENT)) break; if (!block || spin(n)) { if (!block) return false; break; } }
    if (l < 9) *(LAS unsigned*)(lb + L_KS + l * 4) = v;
    else if (l < 18) *(LAS f32x2*)(lb + L_KS + 48 + (l - 9) * 8) = f32x2{bflo(v), bfhi(v)};
    wait_lg0(); if (l == 0) *(volatile LAS int*)(lb + L_CNT + C_KS * 4) = 1;
    return true;
}
DI void routed_token(lptr lb, __amdgpu_buffer_rsrc_t rs, int cand, int rows, bool frag, int l) {
    int cur = -1; u32x4 v, x0 = {}, x1 = {};
    for (unsigned n = 0;;) {
        v = ld128c(rs, cand + (l & 31) * 16);
        const bool ok = umax4(v) != SENT;
        const float cr = ok ? __uint_as_float(v[0]) : -INFINITY, gr = wmax(cr);
        const int gl = (int)__builtin_ctzll(__ballot(ok && cr == gr) | (1ull << 31));
        const int s0 = __builtin_amdgcn_readlane(v[1], gl) & (T - 1);
        if (s0 != cur) { cur = s0;
            x0 = ld128c(rs, rows + (frag ? fl_off(s0, l) : (s0 >> 6) * ROWB + l * 16));
            x1 = ld128c(rs, l < 8 ? rows + (frag ? fl_off(s0, 64 + l) : (s0 >> 6) * ROWB + (64 + l) * 16) : OOB); }
        if (!__any(!ok) || spin(n)) { if (l == gl) *(LAS u32x4*)(lb + L_MISC) = u32x4{v[0], (unsigned)s0, v[2], v[3]}; break; }
    }
    wait_lg0();
    routed_row(lb, rs, rows, frag, l, x0, x1);
}
template <int I>
DI void side2_step(char* lds, Park& F, const SidePtrs& P, int sel) {
    const int tid = fresh_tid(), l = tid & 63; const int w = __builtin_amdgcn_readfirstlane(tid >> 6); const int b = blockIdx.x;
    const int tb = (b & 7) + 8 * (b >> 6), hg = (b >> 3) & 7, g = l >> 4;
    lptr lb = (lptr)lds;
    const __amdgpu_buffer_rsrc_t rs = mkrs(P.ws, (unsigned)WS_END);
    const bool ffn = (b >> 3) < 24, mine = (sel >> 6) == tb, helper = tb == 31;
    if constexpr (I == 0) {
        if (ffn) ffn_gu(lb, F, w, l, C_A, C_H);
        const int o0 = ffn ? (int)WS_FFL + ((((b & 7) * 24 + (b >> 3)) * 8 + w) * 12 + 7) * 1024 + l * 16 : OOB;
#pragma unroll
        for (int j = 0; j < 5; ++j) F.v[7 + j] = __builtin_amdgcn_raw_buffer_load_b128(rs, o0, j * 1024, 0);
    }
    if constexpr (I == 1) { if (ffn) ffn_dn(lb, F, rs, (int)WS_YP1 + ((b & 7) * 24 + (b >> 3)) * 2304, w, l, C_H); }
    if constexpr (I == 3) { const int rid = (tb - 24) * 8 + hg;
        if (!ffn && rid < NRED) { unsigned ow; if (ffn_reduce(lb, rs, (int)WS_YP1, rid, tid, w, C_R, ow)) { if (l < 8) st32c(rs, (int)WS_X2SEL + rid * 32 + l * 4, nosent(ow)); } } }
    if constexpr (I == 4) {
        if (helper) {
            lptr wa, wb; wimg_ptrs(lds, 0, l, wa, wb); wa += w * 5120; wb += w * 5120;
            f32x4 acc[5] = {}; float s1 = 0.f, s2 = 0.f;
            const int xo = (int)WS_X2SEL + (32 * w + 8 * g) * 2;
            u32x4 xn = ld128c(rs, xo);
#pragma unroll 1
            for (int i = 0; i < (w < 2 ? 3 : 2); ++i) {
                u32x4 xw = xn;
                for (unsigned n = 0; __any(umax4(xw) == SENT) && !spin(n);) xw = ld128c(rs, xo + i * 512);
                xn = ld128c(rs, i == 0 || w < 2 ? xo + (i + 1) * 512 : OOB);
                const s16x8 xb = __builtin_bit_cast(s16x8, xw);
                { const s16x8 a0 = tr2(wa, wb), a1 = tr2(wa + 32, wb + 32), a2 = tr2(wa + 64, wb + 64);
                  acc[0] = MFMA16(a0, xb, acc[0]); acc[1] = MFMA16(a1, xb, acc[1]); acc[2] = MFMA16(a2, xb, acc[2]); }
                SBAR();
                { const s16x8 a3 = tr2(wa + 96, wb + 96), a4 = tr2(wa + 128, wb + 128);
                  acc[3] = MFMA16(a3, xb, acc[3]); acc[4] = MFMA16(a4, xb, acc[4]); }
#pragma unroll
                for (int j = 0; j < 4; ++j) { s1 = dot2(xw[j], 0x3f803f80u, s1); s2 = dot2(xw[j], xw[j], s2); }
                wa += 40960; wb += 40960;
            }
            s1 = sum4g(s1); s2 = sum4g(s2);
            if ((l & 15) == 0) {
#pragma unroll
                for (int nt = 0; nt < 5; ++nt) *(LAS f32x4*)(lb + L_CS + 320 + (w * 80 + nt * 16 + g * 4) * 4) = acc[nt];
            }
            if (l == 0) *(LAS f32x2*)(lb + L_SP + 4096 + w * 8) = f32x2{s1, s2};
            wait_lg0();
            int old = 0; if (l == 0) old = __hip_atomic_fetch_add((LAS int*)(lb + L_CNT) + C_B, 1, __ATOMIC_RELAXED, __HIP_MEMORY_SCOPE_WORKGROUP);
            if (__builtin_amdgcn_readfirstlane(old) == 7) {
                f32x2 st = {0.f, 0.f};
#pragma unroll
                for (int i = 0; i < 8; ++i) st += *(LAS f32x2*)(lb + L_SP + 4096 + i * 8);
                const float m = st[0] * (1.f / D), c = rsqrtf(fmaxf(st[1] * (1.f / D) - m * m, 0.f) + EPS);
                f32x2 v = {0.f, 0.f}; const int n = l < 40 ? 2 * l : 0;
#pragma unroll
                for (int i = 0; i < 8; ++i) v += *(LAS f32x2*)(lb + L_CS + 320 + (i * 80 + n) * 4);
                const f32x2 cs = *(LAS f32x2*)(lb + L_CS + n * 4); const float sc = n < 72 ? c * QSCALE : c;
                const unsigned qw = nosent(cvtpk((v[0] - m * cs[0]) * sc, (v[1] - m * cs[1]) * sc));
                if (l < 40) st32c(rs, (int)WS_QSEL + hg * 160 + l * 4, qw);
                if (hg < 5 && l >= 36 && l < 40) st32c(rs, (int)WS_KVSEL + hg * 16 + (l - 36) * 4, qw);
            }
        }
    }
    if constexpr (I == 5) {
        if (mine && w == 0) {
            unsigned q, x;
            for (unsigned n = 0;;) { q = ld32c(rs, l < 40 ? (int)WS_QSEL + hg * 160 + l * 4 : OOB); x = ld32c(rs, l < 36 ? (int)WS_X2SEL + (hg * 36 + l) * 4 : OOB); if (!__any(umax(q, x) == SENT) || spin(n)) break; }
            if (l < 40) *(LAS unsigned*)(lb + L_Q + (sel & 63) * 160 + l * 4) = q;
            if (l < 36) *(LAS unsigned*)(lb + L_XT + (sel & 63) * 144 + l * 4) = x;
            wait_lg0(); if (l == 0) *(volatile LAS int*)(lb + L_CNT + C_QOK * 4) = 1;
        }
    }
    if constexpr (I >= 5) {
        if (helper) {
            if (I == 5) await8(lb, C_B);
            const char* wo = uptr(kargs()->wo + hg * 144); const unsigned vw = w_voff(w, l);
#pragma unroll
            for (int k = 0; k < 6; ++k) dma_w_piece((unsigned)(uintptr_t)lds + L_W, wo, vw, w, 6 * (I - 5) + k);
        }
    }
    if constexpr (I == 6) { if (w == 0) (void)routed_kv(lb, rs, l, false); }
}
DI void side2(char* lds, int i, Park& F, const SidePtrs& P, int sel) {
    if (i == 1) side2_step<1>(lds, F, P, sel); else if (i == 2) side2_step<2>(lds, F, P, sel); else if (i == 3) side2_step<3>(lds, F, P, sel);
    else if (i == 4) side2_step<4>(lds, F, P, sel); else if (i == 5) side2_step<5>(lds, F, P, sel); else side2_step<6>(lds, F, P, sel);
}

DI void block_of(int i, int w, bool special, int tqs, int& qb, int& seq) {
    if (!special) { qb = (w + i) & 7; seq = i; return; }
    if (i < 6) { int c = w + i; c = c >= 12 ? c - 12 : (c >= 6 ? c - 6 : c); const int tq = c >> 1; qb = 2 * (tq + (tq >= tqs ? 1 : 0)) + (c & 1);
        seq = i + (c < i ? 1 : 0) + ((c == 0 ? 5 : c - 1) < i ? 1 : 0) + (w >= 6 ? 1 : 0); }
    else { qb = 2 * tqs + (((w >> 2) + i) & 1); seq = (i - 6) * 4 + (w & 3); }
}
template <int LAYER>
DI void attention(char* lds, Park& F) {
    const int tid = fresh_tid(), l = tid & 63; const int w = __builtin_amdgcn_readfirstlane(tid >> 6);
    const int r32 = l & 31, hi = l >> 5;
    lptr lb = (lptr)lds;
    s16x8 k1[8], v0[8], v1[8]; s16x4 k2[8];
    SidePtrs P; int sel = 0; bool special = false; int tqs = 0;
    const KP p = kargs(); const int b = blockIdx.x;
    { const int f = (b & 7) * 24 + (b >> 3);
      P.ws = (char*)uptr(p->ws);
      if (LAYER == 0) { P.wg = uptr(p->wg + f * (FCOL * 2)); P.wu = uptr(p->wu + f * (FCOL * 2)); P.wd = uptr(p->wd + (size_t)f * FCOL * ROWB); } else { P.wg = P.wu = P.wd = nullptr; } }
    const __amdgpu_buffer_rsrc_t rkv = mkrs(P.ws + (LAYER ? WS_KV2 : WS_KV1), 64 * TILE_B);
    const int tb0 = w * 8 * TILE_B;
    const int offK1 = tb0 + l * 16, offK2 = hi ? OOB : tb0 + 1024 + r32 * 8, offV = r32 < 20 ? tb0 + 1280 + hi * 320 + r32 * 16 : OOB;
#define KVLOAD(t) do { \
          k1[t] = __builtin_bit_cast(s16x8, __builtin_amdgcn_raw_buffer_load_b128(rkv, offK1, (t) * TILE_B, SC1)); k2[t] = __builtin_bit_cast(s16x4, __builtin_amdgcn_raw_buffer_load_b64(rkv, offK2, (t) * TILE_B, SC1)); \
          v0[t] = __builtin_bit_cast(s16x8, __builtin_amdgcn_raw_buffer_load_b128(rkv, offV, (t) * TILE_B, SC1)); v1[t] = __builtin_bit_cast(s16x8, __builtin_amdgcn_raw_buffer_load_b128(rkv, offV, (t) * TILE_B + 640, SC1)); } while (0)
    int seltile = -1;
#define KV_AS_PUBLISHED(SEEN) do { \
        const int q_ = (l * 13) >> 6, h2_ = l - q_ * 5, t2_ = 4 * w + (q_ & 3); \
        const int hoff = l >= 20 ? OOB : (LAYER == 0 ? (int)WS_FLAG + ((t2_ & 7) + 8 * h2_ + 64 * (t2_ >> 3)) * 128 \
                       : tb0 + ((q_ & 3) * 2 + 1) * TILE_B + (h2_ < 2 ? h2_ * 512 + 496 : (h2_ == 2 ? 1024 + 248 : 1280 + 640 + 320 + (h2_ == 3 ? 13 : 17) * 16))); \
        unsigned long long m_ = 0; \
        _Pragma("unroll") for (int j_ = 0; j_ < 4; ++j_) { \
            for (unsigned n_ = 0; ((m_ >> (5 * j_)) & 31) != 31 && n_ <= SPIN_CAP; ++n_) { if (n_) __builtin_amdgcn_s_sleep(1); m_ = __ballot(SEEN); } \
            KVLOAD(2 * j_); KVLOAD(2 * j_ + 1); } } while (0)
    if (LAYER == 1) {
        const __amdgpu_buffer_rsrc_t rw = mkrs(P.ws, (unsigned)WS_END);
        if (w == 0) routed_token(lb, rw, (int)WS_CAND1, (int)WS_X1, true, l);
        KV_AS_PUBLISHED(__builtin_amdgcn_raw_buffer_load_b32(rkv, hoff, 0, SC1) != SENT);
        {
            const int o0 = (b >> 3) < 24 ? (int)WS_FFL + (((b & 7) * 24 + (b >> 3)) * 8 + w) * 12 * 1024 + l * 16 : OOB;
#pragma unroll
            for (int j = 0; j < 7; ++j) F.v[j] = __builtin_amdgcn_raw_buffer_load_b128(rw, o0, j * 1024, 0); }
        bar();
        sel = __builtin_amdgcn_readfirstlane(__float_as_int(*(LAS float*)(lb + L_MISC + 4)));
        special = (sel >> 6) == ((b & 7) + 8 * (b >> 6)); tqs = (sel & 63) >> 4;
        if ((sel >> 8) == w) seltile = (sel >> 5) & 7;
    } else {
        const u32x2 tg = *(LAS u32x2*)(lb + L_MISC + 32); const __amdgpu_buffer_rsrc_t rw = mkrs(P.ws, (unsigned)WS_END);
        KV_AS_PUBLISHED(([&] { const u32x2 f = ld64c(rw, hoff); return f[0] == tg[0] && f[1] == tg[1]; })());
        bar();
    }
#define READY(t) do { \
        if (LAYER == 1) { for (unsigned n_ = 0;;) { const u32x2 c_ = __builtin_bit_cast(u32x2, k2[t]); \
            const unsigned m_ = umax(umax(umax4(__builtin_bit_cast(u32x4, k1[t])), umax(c_[0], c_[1])), umax(umax4(__builtin_bit_cast(u32x4, v0[t])), umax4(__builtin_bit_cast(u32x4, v1[t])))); \
            if (__builtin_expect(!__any(m_ == SENT), 1) || spin(n_)) break; \
            const int l = fresh_tid() & 63, r32 = l & 31, hi = l >> 5; \
            const int offK1 = tb0 + l * 16, offK2 = hi ? OOB : tb0 + 1024 + r32 * 8, offV = r32 < 20 ? tb0 + 1280 + hi * 320 + r32 * 16 : OOB; KVLOAD(t); } } \
        u32x2 c2_ = __builtin_bit_cast(u32x2, k2[t]); c2_[0] = hi ? 0x3f803f80u : c2_[0]; c2_[1] = hi ? 0x00003f80u : c2_[1]; \
        if (LAYER == 1 && seltile == (t)) c2_[1] = l == (sel & 31) ? 0x0000c6eau : c2_[1]; \
        k2[t] = __builtin_bit_cast(s16x4, c2_); } while (0)
    STAMP(33 + 12 * LAYER);
    volatile LAS int* cnt = (volatile LAS int*)(lb + L_CNT);
    lptr qp1 = lb + L_Q + (r32 & 15) * 160 + (r32 >> 4) * 36 + hi * 16, qp2 = hi ? lb + L_QM + r32 * 8 : qp1 + 32;
    lptr accb = lb + L_ACC + l * 16;
    f32x16 o;
    const f32x16 zero16 = {};
#define QOFF(qb) (((qb) >> 1) * 2560 + ((qb) & 1) * 72)
#define LOADQ(A, B, qb) do { lptr a_ = qp1 + QOFF(qb), c_ = qp2 + (hi ? (qb) * 256 : QOFF(qb)); A = __builtin_bit_cast(s16x8, u32x4{*(LAS unsigned*)a_, *(LAS unsigned*)(a_ + 4), *(LAS unsigned*)(a_ + 8), *(LAS unsigned*)(a_ + 12)}); \
        B = __builtin_bit_cast(s16x4, u32x2{*(LAS unsigned*)c_, *(LAS unsigned*)(hi ? c_ + 4 : lb + L_CNT + C_ONE * 4)}); } while (0)
#define LOADQ_R(A, B, qb) do { int qr_ = (qb); asm volatile("" : "+s"(qr_)); LOADQ(A, B, qr_); } while (0)
#define EX(v) __builtin_amdgcn_exp2f(v)
#define PRIO(t) do { if ((t) == 0) __builtin_amdgcn_s_setprio(0); if ((t) == 2) __builtin_amdgcn_s_setprio(1); if ((t) == 4) __builtin_amdgcn_s_setprio(2); if ((t) == 6) __builtin_amdgcn_s_setprio(3); } while (0)
#define STEPH(C, N, t, KN, VPREV, OCIN, HOOK) do { \
        PRIO(t); \
        N = MFMA32(k1[KN], q1, zero16); SBAR(); \
        C[0] = EX(C[0]); C[1] = EX(C[1]); C[2] = EX(C[2]); C[3] = EX(C[3]); p0[0] = cvtpk(C[0], C[1]); p0[1] = cvtpk(C[2], C[3]); SBAR(); \
        N = MFMA32K8(k2[KN], q2, N); SBAR(); \
        HOOK; \
        C[4] = EX(C[4]); C[5] = EX(C[5]); C[6] = EX(C[6]); C[7] = EX(C[7]); p0[2] = cvtpk(C[4], C[5]); p0[3] = cvtpk(C[6], C[7]); SBAR(); \
        if ((t) > 0) o = MFMA32(VPREV, __builtin_bit_cast(s16x8, p1), o); SBAR(); \
        C[8] = EX(C[8]); C[9] = EX(C[9]); C[10] = EX(C[10]); C[11] = EX(C[11]); p1[0] = cvtpk(C[8], C[9]); p1[1] = cvtpk(C[10], C[11]); SBAR(); \
        o = MFMA32(v0[t], __builtin_bit_cast(s16x8, p0), OCIN); SBAR(); \
        C[12] = EX(C[12]); C[13] = EX(C[13]); C[14] = EX(C[14]); C[15] = EX(C[15]); p1[2] = cvtpk(C[12], C[13]); p1[3] = cvtpk(C[14], C[15]); SBAR(); \
        } while (0)
#define STEP(C, N, t, KN, VPREV, OCIN) STEPH(C, N, t, KN, VPREV, OCIN, (void)0)
#define STEP6(C, N, VPREV) STEPH(C, N, 6, 7, VPREV, o, LOADQ(q1, q2, qn))
#define XSKIP false
#define SIDE_T0() do { } while (0)
#define SIDE_T1(i) do { } while (0)
#define BLOCK_END(i, FIRST) do { \
            LAS f32x4* a = (LAS f32x4*)(accb + qb * 3072); \
            const int cv = cnt[qb]; asm volatile("" ::: "memory");            \
            f32x4 a0 = *(volatile LAS f32x4*)(a), a1 = *(volatile LAS f32x4*)(a + 64), a2 = *(volatile LAS f32x4*)(a + 128); \
            SBAR(); \
            o = MFMA32(v1[7], __builtin_bit_cast(s16x8, p1), o); \
            if (seq > 0) { \
                if (__builtin_expect(__builtin_amdgcn_readfirstlane(cv) != seq, 0)) { \
                    lds_wait_ge(cnt + qb, seq); \
                    a0 = *(volatile LAS f32x4*)(a); a1 = *(volatile LAS f32x4*)(a + 64); a2 = *(volatile LAS f32x4*)(a + 128); } \
                _Pragma("unroll") for (int r = 0; r < 4; ++r) { o[r] += a0[r]; o[4 + r] += a1[r]; o[8 + r] += a2[r]; } } \
            if ((i) < 7 || seq < 7) { \
                *(volatile LAS f32x4*)(a) = f32x4{o[0], o[1], o[2], o[3]}; *(volatile LAS f32x4*)(a + 64) = f32x4{o[4], o[5], o[6], o[7]}; *(volatile LAS f32x4*)(a + 128) = f32x4{o[8], o[9], o[10], o[11]}; \
                if (l == 0) cnt[qb] = seq + 1; }           \
            if ((i) < 7) { \
                qb = qn; seq = seqn; \
                SIDE_T0(); \
                if (XSKIP || !((jobs >> (i)) & 1)) { } else if (FIRST) { if (LAYER == 0) side1_step<0>(lds, P, w, l); else side2_step<0>(lds, F, P, sel); } \
                else if (pass == 0) { if (LAYER == 0) side1(lds, i, P, w, l); else side2(lds, i, F, P, sel); } \
                SIDE_T1(i); \
                if (LAYER == 1 && special && (i) == 5 && pass == 0 && !XSKIP) {         \
                    lds_wait_ge(cnt + C_QOK, 1); LOADQ_R(q1, q2, qb); sA = MFMA32(k1[0], q1, zero16); sA = MFMA32K8(k2[0], q2, sA); } \
            } \
        } while (0)
    int qb, seq, qn, seqn;
    const int tbw = (b & 7) + 8 * (b >> 6);
    const unsigned jobs = LAYER == 0 ? ((tbw < 24 ? 0x7f : 0x40) | (w == 0 ? 0x10 : 0))
                                     : (1 | (tbw < 24 ? 2 : 0) | (tbw >= 24 && tbw < 29 ? 8 : 0) | (tbw == 31 ? 0x70 : 0) | (special && w == 0 ? 0x20 : 0) | (w == 0 ? 0x40 : 0));
    s16x8 q1; s16x4 q2; f32x16 sA, sB; u32x4 p0, p1;
    {
        const int pass = 0;
        block_of(0, w, special, tqs, qb, seq); block_of(1, w, special, tqs, qn, seqn);
        WSTAMP(0 + 4 * LAYER, __builtin_readcyclecounter()); WSTAMP(1 + 4 * LAYER, __builtin_amdgcn_s_memrealtime());
        LOADQ(q1, q2, qb);
        READY(0);
        STAMP(54 + 4 * (1 - LAYER));
        sA = MFMA32(k1[0], q1, zero16); sA = MFMA32K8(k2[0], q2, sA);
#define WOP(i) do { if (LAYER == 0) { dma_w_piece((unsigned)(uintptr_t)lds + L_W, wo_, vw_, w, 2 * (i)); dma_w_piece((unsigned)(uintptr_t)lds + L_W, wo_, vw_, w, 2 * (i) + 1); } } while (0)
        const char* wo_ = LAYER == 0 ? uptr(p->wo + ((b >> 3) & 7) * 144) : nullptr; const unsigned vw_ = LAYER == 0 ? w_voff(w, l) : 0u;
        READY(1); STEP(sA, sB, 0, 1, v1[0], zero16); WOP(0);
        READY(2); STEP(sB, sA, 1, 2, v1[0], o); WOP(1);
        READY(3); STEP(sA, sB, 2, 3, v1[1], o); WOP(2);
        READY(4); STEP(sB, sA, 3, 4, v1[2], o); WOP(3);
        STAMP(55 + 4 * (1 - LAYER));
        READY(5); STEP(sA, sB, 4, 5, v1[3], o); WOP(4);
        READY(6); STEP(sB, sA, 5, 6, v1[4], o); WOP(5);
        READY(7); STEP6(sA, sB, v1[5]);
        STEP(sB, sA, 7, 0, v1[6], o);
#undef WOP
        STAMP(56 + 4 * (1 - LAYER));
        BLOCK_END(0, true);
        STAMP(57 + 4 * (1 - LAYER));
    }
#pragma unroll 1
    for (int pass = 0; pass < 2; ++pass) {
        if (pass == 1) {
            block_of(0, w, special, tqs, qb, seq); qb = __builtin_amdgcn_readfirstlane(qb);
            LOADQ_R(q1, q2, qb);
            sA = MFMA32(k1[0], q1, zero16); sA = MFMA32K8(k2[0], q2, sA);
        }
#pragma unroll 1
        for (int i = 1 - pass; i < 8; ++i) {
            block_of(i + 1, w, special, tqs, qn, seqn); qn &= 7;
            STEP(sA, sB, 0, 1, v1[0], zero16);
            STEP(sB, sA, 1, 2, v1[0], o);
            STEP(sA, sB, 2, 3, v1[1], o);
            STEP(sB, sA, 3, 4, v1[2], o);
            STEP(sA, sB, 4, 5, v1[3], o);
                STEP(sB, sA, 5, 6, v1[4], o);
            STEP6(sA, sB, v1[5]);
            STEP(sB, sA, 7, 0, v1[6], o);
            STAMP(34 + 12 * LAYER + i);
            BLOCK_END(i, false);
        }
        WSTAMP(2 + 4 * LAYER, __builtin_readcyclecounter()); WSTAMP(3 + 4 * LAYER, __builtin_amdgcn_s_memrealtime());
        if (LAYER == 1 && special && seq != 7) {
            const int c = (w >> 2) * 3 + (w & 3), tq = c >> 1; qb = 2 * (tq + (tq >= tqs ? 1 : 0)) + (c & 1);
            lds_wait_ge(cnt + qb, 8);
            LAS f32x4* a = (LAS f32x4*)(accb + qb * 3072);
            const f32x4 a0 = *(volatile LAS f32x4*)(a), a1 = *(volatile LAS f32x4*)(a + 64), a2 = *(volatile LAS f32x4*)(a + 128);
#pragma unroll
            for (int r = 0; r < 4; ++r) { o[r] = a0[r]; o[4 + r] = a1[r]; o[8 + r] = a2[r]; }
        }
        if (LAYER == 0 && !XSKIP) await8(lb, C_S5);
        if (LAYER == 1) {
            if (!XSKIP) { if (w == 0 && cnt[C_KS] == 0) (void)routed_kv(lb, mkrs(P.ws, (unsigned)WS_END), l, true);
            lds_wait_ge(cnt + C_KS, 1); }
            qb = __builtin_amdgcn_readfirstlane(qb); LOADQ_R(q1, q2, qb);
            const u32x4 qa = __builtin_bit_cast(u32x4, q1), kw = *(LAS u32x4*)(lb + L_KS + hi * 16); const u32x2 qc = __builtin_bit_cast(u32x2, q2);
            const unsigned k8 = *(LAS unsigned*)(lb + L_KS + 32);
            float s = dot2(qa[0], kw[0], 0.f); s = dot2(qa[1], kw[1], s); s = dot2(qa[2], kw[2], s); s = dot2(qa[3], kw[3], s);
            s = dot2(qc[0], hi ? 0x3f803f80u : k8, s); s = dot2(qc[1], hi ? 0x00003f80u : 0u, s);
            const auto ss = __builtin_amdgcn_permlane32_swap(__float_as_uint(s), __float_as_uint(s), false, false);
            const float pk = rbf(EX(__uint_as_float(ss[0]) + __uint_as_float(ss[1])));
            const f32x4 va = *(LAS f32x4*)(lb + L_KS + 48 + hi * 16), vb = *(LAS f32x4*)(lb + L_KS + 80 + hi * 16); const f32x2 vc = *(LAS f32x2*)(lb + L_KS + 112);
#pragma unroll
            for (int r = 0; r < 4; ++r) { o[r] += pk * va[r]; o[4 + r] += pk * vb[r]; }
            o[8] += pk * vc[0]; o[9] += pk * vc[1]; o[10] += pk;
        }
        const auto sw = __builtin_amdgcn_permlane32_swap(__float_as_uint(o[10]), __float_as_uint(o[10]), false, false);
        const float lsum = __uint_as_float(sw[0]);
        float mag = 0.f;
#pragma unroll
        for (int r = 0; r < 10; ++r) if (r < 8 || !hi) mag = fmaxf(mag, fabsf(o[r])) + 0.f * o[r];
        const bool bad = !(lsum >= 8.6736174e-19f && lsum <= 1.2676506e30f) || !(mag <= 3.0e38f);
        if (pass == 0 && __any(bad) && l == 0) cnt[C_FALL] = 1;
        { const float inv = __builtin_amdgcn_rcpf(lsum); const int te = fresh_tid();
          lptr os = lb + L_OST + ((qb >> 1) * 16 + (te & 15)) * 144 + ((qb & 1) * 2 + ((te >> 4) & 1)) * 36 + ((te >> 5) & 1) * 8;
          *(LAS unsigned*)(os) = cvtpk(o[0] * inv, o[1] * inv); *(LAS unsigned*)(os + 4) = cvtpk(o[2] * inv, o[3] * inv);
          *(LAS unsigned*)(os + 16) = cvtpk(o[4] * inv, o[5] * inv); *(LAS unsigned*)(os + 20) = cvtpk(o[6] * inv, o[7] * inv);
          if (!(te & 32)) *(LAS unsigned*)(os + 32) = cvtpk(o[8] * inv, o[9] * inv); }
        wait_vm0();
        bar();
        if (pass == 1 || __builtin_amdgcn_readfirstlane(cnt[C_FALL]) == 0) break;
        const int tf = fresh_tid();
        LAS int* mx = (LAS int*)(lb + L_SP);
        if (tf < 256) mx[tf] = (int)0x80000000;
        if (tf < 8) cnt[tf] = 0;
        bar();
#pragma unroll 1
        for (int q = 0; q < 8; ++q) {
            LOADQ_R(q1, q2, q);
            float m = -INFINITY;
#pragma unroll
            for (int t = 0; t < 8; ++t) { f32x16 s = MFMA32(k1[t], q1, zero16); s = MFMA32K8(k2[t], q2, s);
#pragma unroll
                for (int r = 0; r < 16; ++r) m = fmaxf(m, s[r]);
                asm volatile("" : "+v"(m)); SBAR(); }
            if (LAYER == 1) {
                const u32x4 qa = __builtin_bit_cast(u32x4, q1), kw = *(LAS u32x4*)(lb + L_KS + hi * 16); const u32x2 qc = __builtin_bit_cast(u32x2, q2);
                float s = dot2(qa[0], kw[0], 0.f); s = dot2(qa[1], kw[1], s); s = dot2(qa[2], kw[2], s); s = dot2(qa[3], kw[3], s);
                if (!hi) s = dot2(qc[0], *(LAS unsigned*)(lb + L_KS + 32), s);
                const auto ss = __builtin_amdgcn_permlane32_swap(__float_as_uint(s), __float_as_uint(s), false, false);
                m = fmaxf(m, __uint_as_float(ss[0]) + __uint_as_float(ss[1]));
            }
            int im = __float_as_int(m); im = im >= 0 ? im : im ^ 0x7fffffff;
            __hip_atomic_fetch_max(mx + q * 32 + (tf & 31), im, __ATOMIC_RELAXED, __HIP_MEMORY_SCOPE_WORKGROUP);
        }
        bar();
        if (tf < 256) { int im = mx[tf]; im = im >= 0 ? im : im ^ 0x7fffffff; const float m = -__int_as_float(im);
            const float m0 = rbf(m), m1 = rbf(m - m0), m2 = rbf(m - m0 - m1);
            *(LAS u32x2*)(lb + L_QM + tf * 8) = u32x2{cvtpk(m0, m1), cvtpk(m2, 0.f)}; }
        bar();
    }
#undef STEP
#undef STEPH
#undef STEP6
#undef LOADQ
#undef KVLOAD
#undef READY
#undef BLOCK_END
}

DI void fix_frags(u32x4 (&xf)[9], __amdgpu_buffer_rsrc_t rs, int off) {
    unsigned m = 0;
#pragma unroll
    for (int s = 0; s < 9; ++s) m = umax(m, umax4(xf[s]));
    if (__builtin_expect(__any(m == SENT), 0)) {
        for (unsigned n = 0; !spin(n);) {
            m = 0;
#pragma unroll
            for (int s = 0; s < 9; ++s) { xf[s] = ld128c(rs, off + s * 1024); }
#pragma unroll
            for (int s = 0; s < 9; ++s) m = umax(m, umax4(xf[s]));
            if (!__any(m == SENT)) break;
        }
    }
}

template <int LAYER>
DI void out_proj(char* lds, Park& Z) {
    IDS();
    constexpr size_t oO = LAYER ? WS_O2 : WS_O1, oS = LAYER ? WS_ST2 : WS_ST1, oC = LAYER ? WS_CAND2 : WS_CAND1;
    st128c(rs, (int)oO + fl_tile(tb, hg, w, l), nosent4(*(LAS u32x4*)(lb + L_OST + l * 144 + w * 16)));
    if (w == 0) st128c(rs, (int)oO + fl_tile(tb, hg, 8, l), nosent4(*(LAS u32x4*)(lb + L_OST + l * 144 + 128)));
    u32x4 xf[9];
    if (LAYER == 0) {
        const int c = tid, r = (c * 7282) >> 16;
        glds16(p->x + tile0 + chunk_off(c), (unsigned)__builtin_amdgcn_readfirstlane(lds0 + L_XT + w * 1024));
        if (w == 0) glds16(p->x + tile0 + chunk_off(512 + l), (unsigned)__builtin_amdgcn_readfirstlane(lds0 + L_XT + 8192)); (void)r;
    }
    STAMP(6 + 16 * LAYER);
    { const int cc = (kh * 4 + (l & 3)) * 9 + 4; hint_wait(rs, l < 4 ? (int)oO + (tb * 4 + mt) * 18432 + (cc >> 2) * 1024 + (cc & 3) * 256 + 240 : OOB); }
#pragma unroll
    for (int s = 0; s < 9; ++s) xf[s] = ld128c(rs, (int)oO + frag0 + s * 1024);
    fix_frags(xf, rs, (int)oO + frag0);
    STAMP(7 + 16 * LAYER);
    lptr wa, wb; wimg_ptrs(lds, kh, l, wa, wb);
    f32x4 acc[5] = {};
    gemm_half(acc, xf, wa, wb);
    KSEND(acc);
    wait_vm0();
    bar();
    STAMP(8 + 16 * LAYER);
    f32x4 own[3]; KRECV(acc, own);
    float s1 = 0.f, s2 = 0.f, s3 = 0.f;
    lptr xt = lb + L_XT + (mt * 16 + tl) * 144 + (kh * 48 + g * 4) * 2;
#pragma unroll
    for (int i = 0; i < 3; ++i) {
        const int n = kh * 48 + i * 16 + g * 4; const bool v = n < 72 && (i < 2 || kh == 0);
        if (v) {
            const u32x2 xw = *(LAS u32x2*)(xt + i * 32);
            const float x4[4] = {bflo(xw[0]), bfhi(xw[0]), bflo(xw[1]), bfhi(xw[1])};
            const u32x2 ww = *(LAS u32x2*)(lb + L_WR + n * 2);
            const float wr4[4] = {bflo(ww[0]), bfhi(ww[0]), bflo(ww[1]), bfhi(ww[1])};
            float xv[4];
#pragma unroll
            for (int r = 0; r < 4; ++r) { xv[r] = rbf(x4[r] + rbf(own[i][r])); s1 += xv[r]; s2 += xv[r] * xv[r]; s3 += xv[r] * wr4[r]; }
            const u32x2 pk = {cvtpk(xv[0], xv[1]), cvtpk(xv[2], xv[3])};
            *(LAS u32x2*)(xt + i * 32) = pk;
            if (LAYER == 0) st64c(rs, (int)WS_X1 + fl_off(tok, hg * 9 + kh * 6 + i * 2 + (g >> 1)) + (g & 1) * 8, u32x2{nosent(pk[0]), nosent(pk[1])});
        }
    }
    s1 = sum4g(s1); s2 = sum4g(s2); s3 = sum4g(s3);
    if (g == 0) st128c(rs, (int)oS + ((tb * 8 + hg) * 128 + kh * 64 + mt * 16 + tl) * 16, u32x4{nosent(__float_as_uint(s1)), nosent(__float_as_uint(s2)), nosent(__float_as_uint(s3)), 0u});
    STAMP(9 + 16 * LAYER);
    const int so = (int)oS + ((tb * 8 + w) * 128 + l) * 16;
    u32x4 sa = ld128c(rs, so), sb = ld128c(rs, so + 1024);
    Park X;
    if (LAYER == 0) { img_get(X, lb, w, l); img_put(Z, lb, w, l); }
    for (unsigned n = 0; __any(umax(umax4(sa), umax4(sb)) == SENT) && n < 16; ++n) { hint_wait(rs, l < 2 ? so + l * 1008 + 1008 : OOB); sa = ld128c(rs, so); sb = ld128c(rs, so + 1024); }
    if (LAYER == 0) {
#pragma unroll
        for (int s = 0; s < 9; ++s) xf[s] = ld128c(rs, (int)WS_X1 + frag0 + s * 1024);
    }
    *(LAS f32x4*)(lb + L_SP + (w * 64 + l) * 16) = __builtin_bit_cast(f32x4, sa) + __builtin_bit_cast(f32x4, sb);
    bar();
    STAMP(10 + 16 * LAYER);
#define ROUTER() do { \
        float t1 = 0.f, t2 = 0.f, t3 = 0.f; \
        _Pragma("unroll") for (int i = 0; i < 8; ++i) { const f32x4 a = *(LAS f32x4*)(lb + L_SP + (i * 64 + l) * 16); t1 += a[0]; t2 += a[1]; t3 += a[2]; } \
        const float mean_ = t1 * (1.f / D), var_ = fmaxf(t2 * (1.f / D) - mean_ * mean_, 0.f), gg_ = rsqrtf(var_ + EPS); \
        const float r = gg_ * (t3 - mean_ * *(LAS float*)(lb + L_MISC + 16)); \
        const float br = wmax(r); \
        const int bl = (int)__builtin_ctzll(__ballot(r == br) | (1ull << 63)); \
        if (LAYER == 1 && l < 9) st128c(rs, (int)WS_CROW + tb * ROWB + hg * 144 + l * 16, nosent4(*(LAS u32x4*)(lb + L_XT + bl * 144 + l * 16))); \
        if (hg == 0 && l == bl) st128c(rs, (int)oC + tb * 16, u32x4{nosent(__float_as_uint(r)), (unsigned)(tb * 64 + l), nosent(__float_as_uint(mean_)), nosent(__float_as_uint(gg_))}); \
        if (LAYER == 1) *(LAS f32x4*)(lb + L_STAT + l * 16) = f32x4{mean_, gg_, r, 0.f}; \
    } while (0)
    if (LAYER == 1) { if (w == 0) ROUTER(); }
    if (LAYER == 0) {
        float t1 = 0.f, t2 = 0.f;
#pragma unroll
        for (int i = 0; i < 8; ++i) { const f32x2 a = *(LAS f32x2*)(lb + L_SP + (i * 64 + mt * 16 + tl) * 16); t1 += a[0]; t2 += a[1]; }
        const float mean = t1 * (1.f / D), var = fmaxf(t2 * (1.f / D) - mean * mean, 0.f), gg = rsqrtf(var + EPS);
        const float c1 = (1.f + gg) * rsqrtf((1.f + gg) * (1.f + gg) * var + EPS);
        fix_frags(xf, rs, (int)WS_X1 + frag0);
        STAMP(18);
#pragma unroll
        for (int nt = 0; nt < 5; ++nt) acc[nt] = f32x4{};
        gemm_half(acc, xf, wa, wb);
        KSEND(acc);
#pragma unroll
        for (int j = 0; j < 12; ++j) Z.v[j] = u32x4{};
        bar();
        KRECV(acc, own);
        proj_epilogue(lds, own, lb + L_CS, mean, c1, mt, kh, l);
        attn_init(lds, tid);
        bar();
        STAMP(19);
        publish_kv(lds, ws + WS_KV2, tb, hg, tid);
        if (w == 2) ROUTER();
        if (tb != 31) img_put(X, lb, w, l);
#pragma unroll
        for (int i = 0; i < 3; ++i) {
            const int n = kh * 48 + i * 16 + g * 4; const bool v = n < 72 && (i < 2 || kh == 0);
            if (v) { const u32x2 x = *(LAS u32x2*)(xt + i * 32);
                *(LAS u32x2*)(xt + i * 32) = u32x2{cvtpk(bflo(x[0]) + rbf((bflo(x[0]) - mean) * gg), bfhi(x[0]) + rbf((bfhi(x[0]) - mean) * gg)),
                                                   cvtpk(bflo(x[1]) + rbf((bflo(x[1]) - mean) * gg), bfhi(x[1]) + rbf((bfhi(x[1]) - mean) * gg))}; } }
    }
#undef ROUTER
}
DI void tail(char* lds, const Park& F) {
    IDS();
    if (w == 0) routed_token(lb, rs, (int)WS_CAND2, (int)WS_CROW, false, l);
    bar();
    STAMP(29);
    const int sel = __builtin_amdgcn_readfirstlane(__float_as_int(*(LAS float*)(lb + L_MISC + 4)));
    if (ffn) ffn_gu(lb, F, w, l, C_A2, C_H2);
#pragma unroll
    for (int h = 0; h < 2; ++h) { const int c = tid + 512 * h;
        if (h == 0 || w == 0) { const int r = (c * 7282) >> 16;
            if (tb * 64 + r != sel) {
                const u32x4 v = *(LAS u32x4*)(lb + L_XT + c * 16); const f32x4 st = *(LAS f32x4*)(lb + L_STAT + r * 16); u32x4 y;
#pragma unroll
                for (int j = 0; j < 4; ++j) y[j] = cvtpk(bflo(v[j]) + rbf((bflo(v[j]) - st[0]) * st[1]), bfhi(v[j]) + rbf((bfhi(v[j]) - st[0]) * st[1]));
                *(u32x4*)(p->out + tile0 + chunk_off(c)) = y;
            } } }
    if (ffn) ffn_dn(lb, F, rs, (int)WS_YP2 + fsl * 2304, w, l, C_H2);
    STAMP(31);
    const int rid = (tb - 24) * 8 + hg;
    if (!ffn && rid < NRED) { unsigned ow; if (ffn_reduce(lb, rs, (int)WS_YP2, rid, tid, w, C_R2, ow)) { if (l < 8) *(unsigned*)(p->out + (size_t)sel * ROWB + rid * 32 + l * 4) = ow; } }
}

__global__ void __launch_bounds__(512, 2) k_main(Params pp) {
    extern __shared__ __attribute__((aligned(16))) char lds[];
    Park Z;
    proj1(lds, Z);
    STAMP(3);
    attention<0>(lds, Z);
    STAMP(5);
    out_proj<0>(lds, Z);
    STAMP(20);
    attention<1>(lds, Z);
    STAMP(21);
    out_proj<1>(lds, Z);
    STAMP(28);
    tail(lds, Z);
    STAMP(32);
}

extern "C" void kernel_launch(void* const* d_in, const int* in_sizes, int n_in, void* d_out, int out_size, void* d_ws, size_t ws_size, hipStream_t stream) {
    Params p;
    p.x = (const char*)d_in[0]; p.wq = (const char*)d_in[1]; p.wk = (const char*)d_in[2]; p.wv = (const char*)d_in[3]; p.wo = (const char*)d_in[4];
    p.wr = (const char*)d_in[6]; p.wg = (const char*)d_in[7]; p.wu = (const char*)d_in[8]; p.wd = (const char*)d_in[9];
    p.out = (char*)d_out; p.ws = (char*)d_ws;
    (void)hipFuncSetAttribute((const void*)k_main, hipFuncAttributeMaxDynamicSharedMemorySize, LDS_BYTES);
    hipLaunchKernelGGL(k_main, dim3(256), dim3(512), LDS_BYTES, stream, p);
}
```
